```python
import torch

def _softmax(x, axis=-1):
    return torch.softmax(x, dim=axis)
def _gelu(x):
    return torch.nn.functional.gelu(x, approximate="tanh")
def _silu(x):
    return torch.nn.functional.silu(x)
def _sigmoid(x):
    return torch.sigmoid(x)
def _layernorm(x, axis=-1, eps=1e-5):
    m = x.mean(dim=axis, keepdim=True)
    v = x.var(dim=axis, keepdim=True, unbiased=False)
    return (x - m) / torch.sqrt(v + eps)
def _scatter_add(tgt, idx, src, axis=0):
    return tgt.clone().index_add_(axis, idx, src)
def _segment_sum(src, seg, n):
    out = torch.zeros((n,) + src.shape[1:], dtype=src.dtype)
    return out.index_add_(0, seg, src)
def _pad(x, pw):
    flat = tuple(v for pair in reversed(pw) for v in pair)
    return torch.nn.functional.pad(x, flat)

kernel_name = 'interleave_layers__linear_attn_block__moe_arch_mod__prompt'

def setup_inputs():

    _w = (lambda a: (lambda t: t.to(torch.bfloat16) if t.is_floating_point() else t)(torch.as_tensor(a)))
    def _A_setup():
        B, S, D = 1, 2048, 1024
        return {
            "x": _w((lambda *s: torch.randn(*s, dtype=torch.bfloat16) * 0.05)(B, S, D)),
            "wqkv": _w((lambda *s: torch.randn(*s, dtype=torch.bfloat16) * 0.05)(D, 3 * D)),
            "wo": _w((lambda *s: torch.randn(*s, dtype=torch.bfloat16) * 0.05)(D, D)),
        }
    _A_in = _A_setup()

    def _B_setup():
        B, S, D, F, C = 1, 2048, 1024, 3584, 8
        return {
            "x": _w((lambda *s: torch.randn(*s, dtype=torch.bfloat16) * 0.05)(B, S, D)),
            "w_r": _w((lambda *s: torch.randn(*s, dtype=torch.bfloat16) * 0.05)(D)),
            "w_gate": _w((lambda *s: torch.randn(*s, dtype=torch.bfloat16) * 0.05)(D, F)),
            "w_up": _w((lambda *s: torch.randn(*s, dtype=torch.bfloat16) * 0.05)(D, F)),
            "w_down": _w((lambda *s: torch.randn(*s, dtype=torch.bfloat16) * 0.05)(F, D)),
        }
    _B_in = _B_setup()

    return {
        "A_x": _A_in["x"],
        "A_wqkv": _A_in["wqkv"],
        "A_wo": _A_in["wo"],
        "B_x": _B_in["x"],
        "B_w_r": _B_in["w_r"],
        "B_w_gate": _B_in["w_gate"],
        "B_w_up": _B_in["w_up"],
        "B_w_down": _B_in["w_down"],
    }

def reference(A_x, A_wqkv, A_wo, B_x, B_w_r, B_w_gate, B_w_up, B_w_down):
    def _A_ref(x, wqkv, wo):
        B, S, D = x.shape
        H = 32; Dh = D // H
        qkv = (lambda x, *d: x.permute(*d))((x @ wqkv).reshape(B, S, 3, H, Dh), 2, 0, 3, 1, 4)
        q, k, v = qkv[0], qkv[1], qkv[2]
        phi = lambda t: torch.where(t > 0, t + 1, torch.exp(t)) + 1e-6
        pq, pk = phi(q), phi(k)
        kv = torch.einsum("bhsd,bhse->bhde", pk, v)
        z = torch.einsum("bhsd->bhd", pk)
        num = torch.einsum("bhqd,bhde->bhqe", pq, kv)
        den = torch.einsum("bhqd,bhd->bhq", pq, z)[..., None] + 1e-6
        o = num / den
        return (lambda x, *d: x.permute(*d))(o, 0, 2, 1, 3).reshape(B, S, D) @ wo

    def _B_ref(x, w_r, w_gate, w_up, w_down):
        B, S, D = x.shape
        C = 8
        r = x @ w_r
        idx = torch.argsort(-r, axis=-1)[:, :C]
        idx_e = (lambda x, n, axis: torch.repeat_interleave(x, n, dim=axis))(idx.reshape(B, C, 1), D, axis=2)
        x_sel = (lambda x, i, axis: torch.take_along_dim(x, i, dim=axis))(x, idx_e, axis=1)
        g = _silu(x_sel @ w_gate)
        u = x_sel @ w_up
        y = (g * u) @ w_down
        r_sel = _sigmoid((lambda x, i, axis: torch.take_along_dim(x, i, dim=axis))(r, idx, axis=1))
        y = y * r_sel.reshape(B, C, 1)
        b_off = (torch.arange(B) * S).reshape(B, 1)
        flat_idx = (idx + b_off).reshape(B * C)
        zero = (x * 0.0).reshape(B * S, D)
        upd = _scatter_add(zero, flat_idx, y.reshape(B * C, D), axis=0)
        return x + upd.reshape(B, S, D)

    x = A_x
    x = x + _A_ref(_layernorm(x, axis=-1), A_wqkv, A_wo)
    x = x + _B_ref(_layernorm(x, axis=-1), B_w_r, B_w_gate, B_w_up, B_w_down)
    x = x + _A_ref(_layernorm(x, axis=-1), A_wqkv, A_wo)
    x = x + _B_ref(_layernorm(x, axis=-1), B_w_r, B_w_gate, B_w_up, B_w_down)
    return x
```

```cpp
#include <hip/hip_runtime.h>
#include <hip/hip_bf16.h>
#include <stdint.h>

typedef unsigned short u16;
using bf16x8 = __attribute__((ext_vector_type(8))) short;
using s16x4 = __attribute__((ext_vector_type(4))) short;
using f32x4 = __attribute__((ext_vector_type(4))) float;
typedef short v4i16_t __attribute__((ext_vector_type(4)));
typedef __attribute__((address_space(3))) const char* lds_cptr;

constexpr int S_ = 2048, D_ = 1024, F_ = 3584;

__device__ __forceinline__ float bf2f(u16 v) { return __uint_as_float((unsigned)v << 16); }
__device__ __forceinline__ u16 f2bf(float f) {
  unsigned u = __float_as_uint(f);
  unsigned r = u + 0x7fffu + ((u >> 16) & 1u);
  return (u16)(r >> 16);
}
__device__ __forceinline__ float rb(float f) { return bf2f(f2bf(f)); }
__device__ __forceinline__ float wave_sum(float v) {
#pragma unroll
  for (int o = 32; o >= 1; o >>= 1) v += __shfl_xor(v, o);
  return v;
}
__device__ __forceinline__ s16x4 vtr(lds_cptr p) {
  return __builtin_bit_cast(s16x4, __builtin_amdgcn_ds_read_tr16_b64_v4i16((__attribute__((address_space(3))) v4i16_t*)p));
}
__device__ __forceinline__ float phi_f(float t) {
  float a = (t > 0.f) ? rb(t + 1.f) : rb(__expf(t));
  return rb(a + 1e-6f);
}

__device__ __forceinline__ void ln_row(const float* x, float* xn) {
  float s = 0.f;
#pragma unroll
  for (int i = 0; i < 16; ++i) s += x[i];
  s = wave_sum(s);
  float mean = s * (1.f / 1024.f);
  float q = 0.f;
#pragma unroll
  for (int i = 0; i < 16; ++i) { float d = x[i] - mean; q += d * d; }
  q = wave_sum(q);
  float var = q * (1.f / 1024.f);
  float m = rb(mean), v = rb(var);
  float sd = rb(sqrtf(rb(v + 1e-5f)));
#pragma unroll
  for (int i = 0; i < 16; ++i) xn[i] = rb(rb(x[i] - m) / sd);
}
__device__ __forceinline__ void load_row(const u16* p, int lane, float* x) {
  bf16x8 a = *(const bf16x8*)(p + lane * 8);
  bf16x8 b = *(const bf16x8*)(p + 512 + lane * 8);
#pragma unroll
  for (int i = 0; i < 8; ++i) { x[i] = bf2f((u16)a[i]); x[8 + i] = bf2f((u16)b[i]); }
}
__device__ __forceinline__ void store_row(u16* p, int lane, const float* x) {
  bf16x8 a, b;
#pragma unroll
  for (int i = 0; i < 8; ++i) { a[i] = (short)f2bf(x[i]); b[i] = (short)f2bf(x[8 + i]); }
  *(bf16x8*)(p + lane * 8) = a;
  *(bf16x8*)(p + 512 + lane * 8) = b;
}

__global__ __launch_bounds__(256) void k_ln(const u16* __restrict__ x, u16* __restrict__ xn) {
  int row = blockIdx.x * 4 + (threadIdx.x >> 6), lane = threadIdx.x & 63;
  float v[16], o[16];
  load_row(x + (size_t)row * D_, lane, v);
  ln_row(v, o);
  store_row(xn + (size_t)row * D_, lane, o);
}

template <int NSEG, int EPI>
__global__ __launch_bounds__(256) void k_gemm(const u16* __restrict__ A, const u16* __restrict__ B, int ldb,
                                              u16* __restrict__ out, const u16* __restrict__ resid,
                                              float* __restrict__ part) {
  constexpr int RB = NSEG * 128;
  constexpr int STAGE = 16384 + 64 * RB;
  constexpr int NT = NSEG * 2;
  extern __shared__ __attribute__((aligned(16))) char smem[];
  const int tid = threadIdx.x, wid = tid >> 6, lane = tid & 63;
  const int wm = wid >> 1, wn = wid & 1, fr = lane & 15, fq = lane >> 4;
  int panel, cg;
  {
    int b = blockIdx.x, xcd = b & 7, i = b >> 3;
    panel = (xcd & 1) * 8 + (i & 7);
    cg = (xcd >> 1) * 4 + (i >> 3);
  }
  const int brow = panel * 128;
  const int bcol = cg * 64;

  const u16* asrc[4];
  const u16* bsrc[NT];
#pragma unroll
  for (int i = 0; i < 4; ++i) {
    int b = (i * 256 + tid) * 16, row = b >> 7, cpos = (b >> 4) & 7;
    asrc[i] = A + (size_t)(brow + row) * 1024 + ((cpos ^ (row & 7)) << 3);
  }
#pragma unroll
  for (int i = 0; i < NT; ++i) {
    int b = (i * 256 + tid) * 16, k = b / RB, o = b % RB, cp = o >> 5, half = (o >> 4) & 1;
    int sw = ((k >> 1) & 1) | (((k >> 3) & 1) << 1);
    int c = (cp & ~3) | ((cp & 3) ^ sw);
    bsrc[i] = B + (size_t)k * ldb + (c >> 2) * 1024 + bcol + (c & 3) * 16 + half * 8;
  }
  auto stage = [&](int buf, int kt) {
    char* sa = smem + buf * STAGE;
    char* sb = sa + 16384;
#pragma unroll
    for (int i = 0; i < 4; ++i)
      __builtin_amdgcn_global_load_lds((const unsigned*)(asrc[i] + kt * 64), (unsigned*)(sa + (i * 256 + tid) * 16), 16, 0, 0);
#pragma unroll
    for (int i = 0; i < NT; ++i)
      __builtin_amdgcn_global_load_lds((const unsigned*)(bsrc[i] + (size_t)kt * 64 * ldb), (unsigned*)(sb + (i * 256 + tid) * 16), 16, 0, 0);
  };

  f32x4 acc[4][NT];
#pragma unroll
  for (int m = 0; m < 4; ++m)
#pragma unroll
    for (int n = 0; n < NT; ++n) acc[m][n] = f32x4{0.f, 0.f, 0.f, 0.f};

  int aoff[4][2];
#pragma unroll
  for (int m = 0; m < 4; ++m)
#pragma unroll
    for (int kk = 0; kk < 2; ++kk) {
      int row = wm * 64 + m * 16 + fr, ch = kk * 4 + fq;
      aoff[m][kk] = row * 128 + ((ch ^ (row & 7)) << 4);
    }
  const int g = lane >> 4, q = (lane & 15) >> 2, p = lane & 3;

  stage(0, 0);
  asm volatile("s_waitcnt vmcnt(0)" ::: "memory");
  __syncthreads();
  for (int kt = 0; kt < 16; ++kt) {
    int cur = kt & 1;
    if (kt + 1 < 16) stage(cur ^ 1, kt + 1);
    const char* sa = smem + cur * STAGE;
    const char* sb = sa + 16384;
#pragma unroll
    for (int kk = 0; kk < 2; ++kk) {
      bf16x8 af[4], bfr[NT];
#pragma unroll
      for (int m = 0; m < 4; ++m) af[m] = *(const bf16x8*)(sa + aoff[m][kk]);
#pragma unroll
      for (int n = 0; n < NT; ++n) {
        int c = (n >> 1) * 4 + wn * 2 + (n & 1);
        s16x4 lo, hi;
        {
          int k = kk * 32 + 8 * g + q;
          int sw = ((k >> 1) & 1) | (((k >> 3) & 1) << 1);
          lo = vtr((lds_cptr)(sb + k * RB + (((c & ~3) | ((c & 3) ^ sw)) << 5) + p * 8));
          k += 4;
          sw = ((k >> 1) & 1) | (((k >> 3) & 1) << 1);
          hi = vtr((lds_cptr)(sb + k * RB + (((c & ~3) | ((c & 3) ^ sw)) << 5) + p * 8));
        }
        bfr[n] = bf16x8{lo[0], lo[1], lo[2], lo[3], hi[0], hi[1], hi[2], hi[3]};
      }
#pragma unroll
      for (int m = 0; m < 4; ++m)
#pragma unroll
        for (int n = 0; n < NT; ++n)
          acc[m][n] = __builtin_amdgcn_mfma_f32_16x16x32_bf16(af[m], bfr[n], acc[m][n], 0, 0, 0);
    }
    asm volatile("s_waitcnt vmcnt(0)" ::: "memory");
    __syncthreads();
  }

  if constexpr (EPI == 0) {
    const int h = cg * 2 + wn;
#pragma unroll
    for (int m = 0; m < 4; ++m)
#pragma unroll
      for (int n = 0; n < 2; ++n)
#pragma unroll
        for (int j = 0; j < 4; ++j) {
          float t = rb(acc[m][n][j]);
          out[(size_t)(brow + wm * 64 + m * 16 + fq * 4 + j) * 1024 + h * 32 + n * 16 + fr] = f2bf(phi_f(t));
        }
    f32x4 kv[2][2];
    float zs[2] = {0.f, 0.f};
#pragma unroll
    for (int a = 0; a < 2; ++a)
#pragma unroll
      for (int b = 0; b < 2; ++b) kv[a][b] = f32x4{0.f, 0.f, 0.f, 0.f};
#pragma unroll
    for (int mp = 0; mp < 2; ++mp) {
      bf16x8 pk[2], vv[2];
#pragma unroll
      for (int t = 0; t < 2; ++t)
#pragma unroll
        for (int hh = 0; hh < 2; ++hh)
#pragma unroll
          for (int j = 0; j < 4; ++j) {
            float kk_ = phi_f(rb(acc[2 * mp + hh][2 + t][j]));
            zs[t] += kk_;
            pk[t][hh * 4 + j] = (short)f2bf(kk_);
            vv[t][hh * 4 + j] = (short)f2bf(acc[2 * mp + hh][4 + t][j]);
          }
#pragma unroll
      for (int a = 0; a < 2; ++a)
#pragma unroll
        for (int b = 0; b < 2; ++b) kv[a][b] = __builtin_amdgcn_mfma_f32_16x16x32_bf16(pk[a], vv[b], kv[a][b], 0, 0, 0);
    }
#pragma unroll
    for (int t = 0; t < 2; ++t) { zs[t] += __shfl_xor(zs[t], 16); zs[t] += __shfl_xor(zs[t], 32); }
    float* red = (float*)smem;
    if (wm == 1) {
#pragma unroll
      for (int a = 0; a < 2; ++a)
#pragma unroll
        for (int b = 0; b < 2; ++b)
#pragma unroll
          for (int j = 0; j < 4; ++j) red[wn * 1056 + (a * 16 + fq * 4 + j) * 32 + b * 16 + fr] = kv[a][b][j];
      if (fq == 0) { red[wn * 1056 + 1024 + fr] = zs[0]; red[wn * 1056 + 1024 + 16 + fr] = zs[1]; }
    }
    __syncthreads();
    if (wm == 0) {
      float* dst = part + ((size_t)panel * 32 + h) * 1056;
#pragma unroll
      for (int a = 0; a < 2; ++a)
#pragma unroll
        for (int b = 0; b < 2; ++b)
#pragma unroll
          for (int j = 0; j < 4; ++j) {
            int idx = (a * 16 + fq * 4 + j) * 32 + b * 16 + fr;
            dst[idx] = kv[a][b][j] + red[wn * 1056 + idx];
          }
      if (fq == 0) {
        dst[1024 + fr] = zs[0] + red[wn * 1056 + 1024 + fr];
        dst[1024 + 16 + fr] = zs[1] + red[wn * 1056 + 1024 + 16 + fr];
      }
    }
  } else {
#pragma unroll
    for (int m = 0; m < 4; ++m)
#pragma unroll
      for (int n = 0; n < 2; ++n)
#pragma unroll
        for (int j = 0; j < 4; ++j) {
          size_t idx = (size_t)(brow + wm * 64 + m * 16 + fq * 4 + j) * 1024 + bcol + wn * 32 + n * 16 + fr;
          out[idx] = f2bf(bf2f(resid[idx]) + rb(acc[m][n][j]));
        }
  }
}

__global__ __launch_bounds__(256) void k_attn_apply(const float* __restrict__ part, const u16* __restrict__ pq,
                                                     u16* __restrict__ o) {
  __shared__ u16 kvs[1056];
  int h = blockIdx.x & 31, chunk = blockIdx.x >> 5;
  int tid = threadIdx.x, wid = tid >> 6, lane = tid & 63, fr = lane & 15, fq = lane >> 4;
  for (int i = tid; i < 1056; i += 256) {
    float s = 0.f;
#pragma unroll
    for (int p = 0; p < 16; ++p) s += part[((size_t)p * 32 + h) * 1056 + i];
    kvs[i] = f2bf(s);
  }
  __syncthreads();
  bf16x8 b0, b1, bz;
#pragma unroll
  for (int j = 0; j < 8; ++j) {
    b0[j] = (short)kvs[(fq * 8 + j) * 32 + fr];
    b1[j] = (short)kvs[(fq * 8 + j) * 32 + 16 + fr];
    bz[j] = (short)kvs[1024 + fq * 8 + j];
  }
#pragma unroll
  for (int m = 0; m < 4; ++m) {
    int row0 = chunk * 256 + wid * 64 + m * 16;
    bf16x8 a = *(const bf16x8*)(pq + (size_t)(row0 + fr) * 1024 + h * 32 + fq * 8);
    f32x4 z4 = {0.f, 0.f, 0.f, 0.f};
    f32x4 n0 = __builtin_amdgcn_mfma_f32_16x16x32_bf16(a, b0, z4, 0, 0, 0);
    f32x4 n1 = __builtin_amdgcn_mfma_f32_16x16x32_bf16(a, b1, z4, 0, 0, 0);
    f32x4 dn = __builtin_amdgcn_mfma_f32_16x16x32_bf16(a, bz, z4, 0, 0, 0);
#pragma unroll
    for (int j = 0; j < 4; ++j) {
      float den = rb(rb(dn[j]) + 1e-6f);
      size_t base = (size_t)(row0 + fq * 4 + j) * 1024 + h * 32 + fr;
      o[base] = f2bf(rb(n0[j]) / den);
      o[base + 16] = f2bf(rb(n1[j]) / den);
    }
  }
}

__global__ __launch_bounds__(256) void k_row(const u16* __restrict__ x1, const u16* __restrict__ w_r,
                                             u16* __restrict__ xn, u16* __restrict__ x2, u16* __restrict__ xn2,
                                             float* __restrict__ r) {
  int row = blockIdx.x * 4 + (threadIdx.x >> 6), lane = threadIdx.x & 63;
  float v[16], o[16], w[16];
  load_row(x1 + (size_t)row * D_, lane, v);
  load_row(w_r, lane, w);
  ln_row(v, o);
  float s = 0.f;
#pragma unroll
  for (int i = 0; i < 16; ++i) s += o[i] * w[i];
  s = wave_sum(s);
  if (lane == 0) r[row] = rb(s);
  store_row(xn + (size_t)row * D_, lane, o);
#pragma unroll
  for (int i = 0; i < 16; ++i) v[i] = rb(v[i] + o[i]);
  store_row(x2 + (size_t)row * D_, lane, v);
  if (xn2) {
    ln_row(v, o);
    store_row(xn2 + (size_t)row * D_, lane, o);
  }
}

__device__ __forceinline__ void wave_top8(const float* __restrict__ r, int lane, int* out) {
  float v[32];
#pragma unroll
  for (int i = 0; i < 32; ++i) v[i] = r[i * 64 + lane];
#pragma unroll 1
  for (int t = 0; t < 8; ++t) {
    float best = v[0]; int bi = 0;
#pragma unroll
    for (int i = 1; i < 32; ++i) if (v[i] > best) { best = v[i]; bi = i; }
    int idx = bi * 64 + lane;
#pragma unroll
    for (int o = 32; o >= 1; o >>= 1) {
      float ob = __shfl_xor(best, o); int oi = __shfl_xor(idx, o);
      if (ob > best || (ob == best && oi < idx)) { best = ob; idx = oi; }
    }
    out[t] = idx;
    if ((idx & 63) == lane) {
#pragma unroll
      for (int i = 0; i < 32; ++i) if (i == (idx >> 6)) v[i] = -INFINITY;
    }
  }
}

__global__ __launch_bounds__(256) void k_gateup(const float* __restrict__ r, const u16* __restrict__ xn,
                                                const u16* __restrict__ wg, const u16* __restrict__ wu,
                                                float* __restrict__ gpart, float* __restrict__ upart,
                                                int* __restrict__ idx_out) {
  __shared__ __attribute__((aligned(16))) char smem[2 * 256 * 128];
  int b = blockIdx.x;
  int cg = b % 56, ks = b / 56;
  int tid = threadIdx.x, wid = tid >> 6, lane = tid & 63, fr = lane & 15, fq = lane >> 4;
#pragma unroll
  for (int i = 0; i < 8; ++i) {
    int bb = (i * 256 + tid) * 16, k = bb >> 7, cp = (bb >> 5) & 3, half = (bb >> 4) & 1;
    int sw = ((k >> 1) & 1) | (((k >> 3) & 1) << 1);
    int c = cp ^ sw;
    size_t src = (size_t)(ks * 256 + k) * F_ + cg * 64 + c * 16 + half * 8;
    __builtin_amdgcn_global_load_lds((const unsigned*)(wg + src), (unsigned*)(smem + bb), 16, 0, 0);
    __builtin_amdgcn_global_load_lds((const unsigned*)(wu + src), (unsigned*)(smem + 32768 + bb), 16, 0, 0);
  }
  int idx[8];
  wave_top8(r, lane, idx);
  if (b == 0 && tid < 8) {
    int v = idx[0];
#pragma unroll
    for (int i = 1; i < 8; ++i) if (tid == i) v = idx[i];
    idx_out[tid] = v;
  }
  int myrow = idx[0];
#pragma unroll
  for (int i = 1; i < 8; ++i) if ((fr & 7) == i) myrow = idx[i];
  f32x4 acc[2][4];
#pragma unroll
  for (int a = 0; a < 2; ++a)
#pragma unroll
    for (int n = 0; n < 4; ++n) acc[a][n] = f32x4{0.f, 0.f, 0.f, 0.f};
  bf16x8 af[2];
#pragma unroll
  for (int kk = 0; kk < 2; ++kk) af[kk] = *(const bf16x8*)(xn + (size_t)myrow * D_ + ks * 256 + wid * 64 + kk * 32 + fq * 8);
  asm volatile("s_waitcnt vmcnt(0)" ::: "memory");
  __syncthreads();
  const int g = lane >> 4, q = (lane & 15) >> 2, p = lane & 3;
#pragma unroll
  for (int kk = 0; kk < 2; ++kk)
#pragma unroll
    for (int a = 0; a < 2; ++a)
#pragma unroll
      for (int n = 0; n < 4; ++n) {
        int k = wid * 64 + kk * 32 + 8 * g + q;
        int sw = ((k >> 1) & 1) | (((k >> 3) & 1) << 1);
        s16x4 lo = vtr((lds_cptr)(smem + a * 32768 + k * 128 + ((n ^ sw) << 5) + p * 8));
        k += 4;
        sw = ((k >> 1) & 1) | (((k >> 3) & 1) << 1);
        s16x4 hi = vtr((lds_cptr)(smem + a * 32768 + k * 128 + ((n ^ sw) << 5) + p * 8));
        bf16x8 bf = bf16x8{lo[0], lo[1], lo[2], lo[3], hi[0], hi[1], hi[2], hi[3]};
        acc[a][n] = __builtin_amdgcn_mfma_f32_16x16x32_bf16(af[kk], bf, acc[a][n], 0, 0, 0);
      }
  __syncthreads();
  float* red = (float*)smem;
  if (fq < 2) {
#pragma unroll
    for (int a = 0; a < 2; ++a)
#pragma unroll
      for (int n = 0; n < 4; ++n)
#pragma unroll
        for (int j = 0; j < 4; ++j) red[((wid * 2 + a) * 8 + fq * 4 + j) * 64 + n * 16 + fr] = acc[a][n][j];
  }
  __syncthreads();
  for (int i = tid; i < 1024; i += 256) {
    int a = i >> 9, row = (i >> 6) & 7, c = i & 63;
    float s = 0.f;
#pragma unroll
    for (int w = 0; w < 4; ++w) s += red[((w * 2 + a) * 8 + row) * 64 + c];
    float* dst = a ? upart : gpart;
    dst[((size_t)ks * 8 + row) * F_ + cg * 64 + c] = s;
  }
}

__global__ __launch_bounds__(256) void k_down(const float* __restrict__ gpart, const float* __restrict__ upart,
                                              const u16* __restrict__ wd, float* __restrict__ ypart) {
  __shared__ __attribute__((aligned(16))) char smem[256 * 128 + 8 * 256 * 2];
  u16* hs = (u16*)(smem + 32768);
  int b = blockIdx.x;
  int cg = b & 15, kc = b >> 4;
  int tid = threadIdx.x, wid = tid >> 6, lane = tid & 63, fr = lane & 15, fq = lane >> 4;
#pragma unroll
  for (int i = 0; i < 8; ++i) {
    int bb = (i * 256 + tid) * 16, k = bb >> 7, cp = (bb >> 5) & 3, half = (bb >> 4) & 1;
    int sw = ((k >> 1) & 1) | (((k >> 3) & 1) << 1);
    int c = cp ^ sw;
    size_t src = (size_t)(kc * 256 + k) * D_ + cg * 64 + c * 16 + half * 8;
    __builtin_amdgcn_global_load_lds((const unsigned*)(wd + src), (unsigned*)(smem + bb), 16, 0, 0);
  }
  for (int i = tid; i < 2048; i += 256) {
    int row = i >> 8, c = i & 255;
    float gs = 0.f, us = 0.f;
#pragma unroll
    for (int s = 0; s < 4; ++s) {
      gs += gpart[((size_t)s * 8 + row) * F_ + kc * 256 + c];
      us += upart[((size_t)s * 8 + row) * F_ + kc * 256 + c];
    }
    gs = rb(gs); us = rb(us);
    float sl = rb(gs / (1.f + __expf(-gs)));
    hs[row * 256 + c] = f2bf(sl * us);
  }
  asm volatile("s_waitcnt vmcnt(0)" ::: "memory");
  __syncthreads();
  f32x4 acc[4];
#pragma unroll
  for (int n = 0; n < 4; ++n) acc[n] = f32x4{0.f, 0.f, 0.f, 0.f};
  const int g = lane >> 4, q = (lane & 15) >> 2, p = lane & 3;
#pragma unroll
  for (int kk = 0; kk < 2; ++kk) {
    bf16x8 af = *(const bf16x8*)(hs + (fr & 7) * 256 + wid * 64 + kk * 32 + fq * 8);
#pragma unroll
    for (int n = 0; n < 4; ++n) {
      int k = wid * 64 + kk * 32 + 8 * g + q;
      int sw = ((k >> 1) & 1) | (((k >> 3) & 1) << 1);
      s16x4 lo = vtr((lds_cptr)(smem + k * 128 + ((n ^ sw) << 5) + p * 8));
      k += 4;
      sw = ((k >> 1) & 1) | (((k >> 3) & 1) << 1);
      s16x4 hi = vtr((lds_cptr)(smem + k * 128 + ((n ^ sw) << 5) + p * 8));
      bf16x8 bf = bf16x8{lo[0], lo[1], lo[2], lo[3], hi[0], hi[1], hi[2], hi[3]};
      acc[n] = __builtin_amdgcn_mfma_f32_16x16x32_bf16(af, bf, acc[n], 0, 0, 0);
    }
  }
  __syncthreads();
  float* red = (float*)smem;
  if (fq < 2) {
#pragma unroll
    for (int n = 0; n < 4; ++n)
#pragma unroll
      for (int j = 0; j < 4; ++j) red[(wid * 8 + fq * 4 + j) * 64 + n * 16 + fr] = acc[n][j];
  }
  __syncthreads();
  for (int i = tid; i < 512; i += 256) {
    int row = i >> 6, c = i & 63;
    float s = red[row * 64 + c] + red[(8 + row) * 64 + c] + red[(16 + row) * 64 + c] + red[(24 + row) * 64 + c];
    ypart[((size_t)kc * 8 + row) * D_ + cg * 64 + c] = s;
  }
}

__global__ __launch_bounds__(64) void k_fix(const float* __restrict__ ypart, const int* __restrict__ idx,
                                            const float* __restrict__ r, const u16* __restrict__ x1,
                                            const u16* __restrict__ xn, u16* __restrict__ x2, u16* __restrict__ xn2) {
  int c = blockIdx.x, lane = threadIdx.x;
  int row = idx[c];
  float rs = rb(1.f / (1.f + __expf(-r[row])));
  float v[16], o[16], y[16];
  load_row(x1 + (size_t)row * D_, lane, v);
  load_row(xn + (size_t)row * D_, lane, o);
#pragma unroll
  for (int i = 0; i < 16; ++i) y[i] = 0.f;
#pragma unroll 1
  for (int kc = 0; kc < 14; ++kc) {
    const float* src = ypart + ((size_t)kc * 8 + c) * D_;
#pragma unroll
    for (int i = 0; i < 8; ++i) { y[i] += src[lane * 8 + i]; y[8 + i] += src[512 + lane * 8 + i]; }
  }
#pragma unroll
  for (int i = 0; i < 16; ++i) {
    float t = rb(rb(y[i]) * rs);
    float bo = rb(o[i] + t);
    v[i] = rb(v[i] + bo);
  }
  store_row(x2 + (size_t)row * D_, lane, v);
  if (xn2) {
    ln_row(v, o);
    store_row(xn2 + (size_t)row * D_, lane, o);
  }
}

extern "C" void kernel_launch(void* const* d_in, const int* in_sizes, int n_in,
                              void* d_out, int out_size, void* d_ws, size_t ws_size,
                              hipStream_t stream) {
  const u16* A_x = (const u16*)d_in[0];
  const u16* wqkv = (const u16*)d_in[1];
  const u16* wo = (const u16*)d_in[2];
  const u16* w_r = (const u16*)d_in[4];
  const u16* wg = (const u16*)d_in[5];
  const u16* wu = (const u16*)d_in[6];
  const u16* wd = (const u16*)d_in[7];
  u16* outp = (u16*)d_out;

  char* ws = (char*)d_ws;
  const size_t MB4 = (size_t)S_ * D_ * 2;
  u16* xn = (u16*)(ws);
  u16* pq = (u16*)(ws + MB4);
  u16* o = (u16*)(ws + 2 * MB4);
  u16* x1 = (u16*)(ws + 3 * MB4);
  u16* x2 = (u16*)(ws + 4 * MB4);
  u16* xnb = (u16*)(ws + 5 * MB4);
  char* p = ws + 6 * MB4;
  float* part = (float*)p; p += (size_t)16 * 32 * 1056 * 4;
  float* r = (float*)p; p += 2048 * 4;
  int* idx = (int*)p; p += 256;
  float* gpart = (float*)p; p += (size_t)4 * 8 * F_ * 4;
  float* upart = (float*)p; p += (size_t)4 * 8 * F_ * 4;
  float* ypart = (float*)p; p += (size_t)14 * 8 * D_ * 4;

  constexpr int LDS1 = 2 * (16384 + 64 * 384);
  constexpr int LDS3 = 2 * (16384 + 64 * 128);
  hipFuncSetAttribute((const void*)k_gemm<3, 0>, hipFuncAttributeMaxDynamicSharedMemorySize, LDS1);

  k_ln<<<512, 256, 0, stream>>>(A_x, xn);
  const u16* xcur = A_x;
  for (int layer = 0; layer < 2; ++layer) {
    k_gemm<3, 0><<<256, 256, LDS1, stream>>>(xn, wqkv, 3072, pq, nullptr, part);
    k_attn_apply<<<256, 256, 0, stream>>>(part, pq, o);
    k_gemm<1, 1><<<256, 256, LDS3, stream>>>(o, wo, 1024, x1, xcur, nullptr);
    u16* xo = (layer == 1) ? outp : x2;
    u16* xn2 = (layer == 1) ? nullptr : xn;
    k_row<<<512, 256, 0, stream>>>(x1, w_r, xnb, xo, xn2, r);
    k_gateup<<<224, 256, 0, stream>>>(r, xnb, wg, wu, gpart, upart, idx);
    k_down<<<224, 256, 0, stream>>>(gpart, upart, wd, ypart);
    k_fix<<<8, 64, 0, stream>>>(ypart, idx, r, x1, xnb, xo, xn2);
    xcur = x2;
  }
}
```

```cpp
#include <hip/hip_runtime.h>
#include <hip/hip_bf16.h>
#include <stdint.h>
#include <type_traits>

typedef unsigned short u16;
using bf16x8 = __attribute__((ext_vector_type(8))) short;
using s16x4 = __attribute__((ext_vector_type(4))) short;
using f32x4 = __attribute__((ext_vector_type(4))) float;
typedef short v4i16_t __attribute__((ext_vector_type(4)));
typedef __attribute__((address_space(3))) const char* lds_cptr;

constexpr int S_ = 2048, D_ = 1024, F_ = 3584;

__device__ __forceinline__ float bf2f(u16 v) { return __uint_as_float((unsigned)v << 16); }
__device__ __forceinline__ u16 f2bf(float f) {
  unsigned u = __float_as_uint(f);
  unsigned r = u + 0x7fffu + ((u >> 16) & 1u);
  return (u16)(r >> 16);
}
__device__ __forceinline__ float rb(float f) { return bf2f(f2bf(f)); }
__device__ __forceinline__ float wave_sum(float v) {
#pragma unroll
  for (int o = 32; o >= 1; o >>= 1) v += __shfl_xor(v, o);
  return v;
}
__device__ __forceinline__ s16x4 vtr(lds_cptr p) {
  return __builtin_bit_cast(s16x4, __builtin_amdgcn_ds_read_tr16_b64_v4i16((__attribute__((address_space(3))) v4i16_t*)p));
}
__device__ __forceinline__ float phi_f(float t) {
  float a = (t > 0.f) ? rb(t + 1.f) : rb(__expf(t));
  return rb(a + 1e-6f);
}

__device__ __forceinline__ void ln_row(const float* x, float* xn) {
  float s = 0.f;
#pragma unroll
  for (int i = 0; i < 16; ++i) s += x[i];
  s = wave_sum(s);
  float mean = s * (1.f / 1024.f);
  float q = 0.f;
#pragma unroll
  for (int i = 0; i < 16; ++i) { float d = x[i] - mean; q += d * d; }
  q = wave_sum(q);
  float var = q * (1.f / 1024.f);
  float m = rb(mean), v = rb(var);
  float sd = rb(sqrtf(rb(v + 1e-5f)));
#pragma unroll
  for (int i = 0; i < 16; ++i) xn[i] = rb(rb(x[i] - m) / sd);
}
__device__ __forceinline__ void load_row(const u16* p, int lane, float* x) {
  bf16x8 a = *(const bf16x8*)(p + lane * 8);
  bf16x8 b = *(const bf16x8*)(p + 512 + lane * 8);
#pragma unroll
  for (int i = 0; i < 8; ++i) { x[i] = bf2f((u16)a[i]); x[8 + i] = bf2f((u16)b[i]); }
}
__device__ __forceinline__ void store_row(u16* p, int lane, const float* x) {
  bf16x8 a, b;
#pragma unroll
  for (int i = 0; i < 8; ++i) { a[i] = (short)f2bf(x[i]); b[i] = (short)f2bf(x[8 + i]); }
  *(bf16x8*)(p + lane * 8) = a;
  *(bf16x8*)(p + 512 + lane * 8) = b;
}

__global__ __launch_bounds__(256) void k_ln(const u16* __restrict__ x, u16* __restrict__ xn) {
  int row = blockIdx.x * 4 + (threadIdx.x >> 6), lane = threadIdx.x & 63;
  float v[16], o[16];
  load_row(x + (size_t)row * D_, lane, v);
  ln_row(v, o);
  store_row(xn + (size_t)row * D_, lane, o);
}

template <int NSEG, int EPI, int NS>
__global__ __launch_bounds__(512) void k_gemm(const u16* __restrict__ A, const u16* __restrict__ B, int ldb,
                                              u16* __restrict__ out, const u16* __restrict__ resid,
                                              float* __restrict__ part) {
  constexpr int RB = NSEG * 128;
  constexpr int STAGE = 16384 + 64 * RB;
  constexpr int NT = NSEG * 2;
  extern __shared__ __attribute__((aligned(16))) char smem[];
  const int tid = threadIdx.x & 255, wid = tid >> 6, lane = tid & 63;
  const bool loader = __builtin_amdgcn_readfirstlane(threadIdx.x) >= 256;
  const int wm = wid >> 1, wn = wid & 1, fr = lane & 15, fq = lane >> 4;
  int panel, cg;
  {
    int b = blockIdx.x, xcd = b & 7, i = b >> 3;
    panel = (xcd & 1) * 8 + (i & 7);
    cg = (xcd >> 1) * 4 + (i >> 3);
  }
  const int brow = panel * 128;
  const int bcol = cg * 64;

  const u16* asrc[4];
  const u16* bsrc[NT];
#pragma unroll
  for (int i = 0; i < 4; ++i) {
    int b = (i * 256 + tid) * 16, row = b >> 7, cpos = (b >> 4) & 7;
    asrc[i] = A + (size_t)(brow + row) * 1024 + ((cpos ^ (row & 7)) << 3);
  }
#pragma unroll
  for (int i = 0; i < NT; ++i) {
    int b = (i * 256 + tid) * 16, k = b / RB, o = b % RB, cp = o >> 5, half = (o >> 4) & 1;
    int sw = ((k >> 1) & 1) | (((k >> 3) & 1) << 1);
    int c = (cp & ~3) | ((cp & 3) ^ sw);
    bsrc[i] = B + (size_t)k * ldb + (c >> 2) * 1024 + bcol + (c & 3) * 16 + half * 8;
  }
  auto stage = [&](int buf, int kt) {
    char* sa = smem + buf * STAGE;
    char* sb = sa + 16384;
#pragma unroll
    for (int i = 0; i < 4; ++i)
      __builtin_amdgcn_global_load_lds((const unsigned*)(asrc[i] + kt * 64), (unsigned*)(sa + (i * 256 + tid) * 16), 16, 0, 0);
#pragma unroll
    for (int i = 0; i < NT; ++i)
      __builtin_amdgcn_global_load_lds((const unsigned*)(bsrc[i] + (size_t)kt * 64 * ldb), (unsigned*)(sb + (i * 256 + tid) * 16), 16, 0, 0);
  };

  f32x4 acc[4][NT];
#pragma unroll
  for (int m = 0; m < 4; ++m)
#pragma unroll
    for (int n = 0; n < NT; ++n) acc[m][n] = f32x4{0.f, 0.f, 0.f, 0.f};

  const int g = lane >> 4, q = (lane & 15) >> 2, p = lane & 3;
  const unsigned lbase = (unsigned)(size_t)smem;
  unsigned abase[2], bbase[2];
#pragma unroll
  for (int kk = 0; kk < 2; ++kk) {
    int row = wm * 64 + fr, ch = kk * 4 + fq;
    abase[kk] = lbase + row * 128 + ((ch ^ (row & 7)) << 4);
  }
  {
    int sw = ((q >> 1) & 1) | ((g & 1) << 1);
#pragma unroll
    for (int t = 0; t < 2; ++t) bbase[t] = lbase + 16384 + (8 * g + q) * RB + (((wn * 2 + t) ^ sw) << 5) + p * 8;
  }

  constexpr int LPS = 4 + NT;
  bf16x8 af[2][4];
  s16x4 bl[2][NT], bh[2][NT];
  auto rd1 = [&](int kk, unsigned aa, unsigned b0, unsigned b1, int i) {
    if (i < 4) {
      asm volatile("ds_read_b128 %0, %1 offset:%2" : "=v"(af[kk][i]) : "v"(aa), "i"(i * 2048) : "memory");
    } else {
      int n = (i - 4) >> 1;
      unsigned bb = (n & 1) ? b1 : b0;
      if (((i - 4) & 1) == 0)
        asm volatile("ds_read_b64_tr_b16 %0, %1 offset:%2" : "=v"(bl[kk][n]) : "v"(bb), "i"(kk * 32 * RB + (n >> 1) * 128) : "memory");
      else
        asm volatile("ds_read_b64_tr_b16 %0, %1 offset:%2" : "=v"(bh[kk][n]) : "v"(bb), "i"(kk * 32 * RB + 4 * RB + (n >> 1) * 128) : "memory");
    }
  };
  auto rd = [&](int kk, unsigned soff) {
    unsigned aa = abase[kk] + soff, b0 = bbase[0] + soff, b1 = bbase[1] + soff;
#pragma unroll
    for (int i = 0; i < 4 + 2 * NT; ++i) rd1(kk, aa, b0, b1, i);
  };
  auto mma_rd = [&](int kk, int kr, unsigned soff) {
    unsigned aa = abase[kr] + soff, b0 = bbase[0] + soff, b1 = bbase[1] + soff;
#pragma unroll
    for (int m = 0; m < 4; ++m)
#pragma unroll
      for (int n = 0; n < NT; ++n) {
        bf16x8 bf = bf16x8{bl[kk][n][0], bl[kk][n][1], bl[kk][n][2], bl[kk][n][3], bh[kk][n][0], bh[kk][n][1], bh[kk][n][2], bh[kk][n][3]};
        acc[m][n] = __builtin_amdgcn_mfma_f32_16x16x32_bf16(af[kk][m], bf, acc[m][n], 0, 0, 0);
        int i = m * NT + n;
        if (i < 4 + 2 * NT) rd1(kr, aa, b0, b1, i);
        __builtin_amdgcn_sched_barrier(0);
      }
    if (4 * NT < 4 + 2 * NT) {
#pragma unroll
      for (int i = 4 * NT; i < 4 + 2 * NT; ++i) rd1(kr, aa, b0, b1, i);
    }
  };
  auto mma = [&](int kk) {
#pragma unroll
    for (int m = 0; m < 4; ++m)
#pragma unroll
      for (int n = 0; n < NT; ++n) {
        bf16x8 bf = bf16x8{bl[kk][n][0], bl[kk][n][1], bl[kk][n][2], bl[kk][n][3], bh[kk][n][0], bh[kk][n][1], bh[kk][n][2], bh[kk][n][3]};
        acc[m][n] = __builtin_amdgcn_mfma_f32_16x16x32_bf16(af[kk][m], bf, acc[m][n], 0, 0, 0);
      }
  };
  if (loader) {
#pragma unroll
    for (int i = 0; i < NS - 1; ++i) stage(i, i);
    asm volatile("s_waitcnt vmcnt(%0)" ::"n"(LPS * (NS - 2)) : "memory");
    __builtin_amdgcn_s_barrier();
    stage(NS - 1, NS - 1);
    auto lstep = [&](int kt, auto do_stage, auto wait_n) {
      asm volatile("s_waitcnt vmcnt(%0)" ::"n"(decltype(wait_n)::value) : "memory");
      __builtin_amdgcn_s_barrier();
      if constexpr (decltype(do_stage)::value) stage((kt + NS) % NS, kt + NS);
    };
#pragma unroll 1
    for (int kt = 0; kt <= 15 - NS; ++kt) lstep(kt, std::true_type{}, std::integral_constant<int, LPS*(NS - 2)>{});
#pragma unroll
    for (int kt = 16 - NS; kt < 15; ++kt) {
      if (kt == 14) lstep(kt, std::false_type{}, std::integral_constant<int, 0>{});
      else if (kt == 13) lstep(kt, std::false_type{}, std::integral_constant<int, LPS>{});
      else if (kt == 12) lstep(kt, std::false_type{}, std::integral_constant<int, 2 * LPS>{});
      else lstep(kt, std::false_type{}, std::integral_constant<int, 3 * LPS>{});
    }
  } else {
    __builtin_amdgcn_s_barrier();
    rd(0, 0);
    auto step = [&](int kt) {
      const unsigned soff = (kt % NS) * STAGE;
      const unsigned soffn = ((kt + 1) % NS) * STAGE;
      asm volatile("s_waitcnt lgkmcnt(0)" ::: "memory");
      __builtin_amdgcn_sched_barrier(0);
      mma_rd(0, 1, soff);
      asm volatile("s_waitcnt lgkmcnt(0)" ::: "memory");
      __builtin_amdgcn_s_barrier();
      __builtin_amdgcn_sched_barrier(0);
      mma_rd(1, 0, soffn);
    };
#pragma unroll 1
    for (int kt = 0; kt < 15; ++kt) step(kt);
    {
      const unsigned soff = (15 % NS) * STAGE;
      asm volatile("s_waitcnt lgkmcnt(0)" ::: "memory");
      __builtin_amdgcn_sched_barrier(0);
      mma_rd(0, 1, soff);
      asm volatile("s_waitcnt lgkmcnt(0)" ::: "memory");
      __builtin_amdgcn_sched_barrier(0);
      mma(1);
    }
  }
  __syncthreads();
  if (loader) return;

  if constexpr (EPI == 0) {
    const int h = cg * 2 + wn;
    u16* qt = (u16*)(smem + 16384) + wid * 64 * 40;
#pragma unroll
    for (int m = 0; m < 4; ++m)
#pragma unroll
      for (int n = 0; n < 2; ++n)
#pragma unroll
        for (int j = 0; j < 4; ++j) {
          float t = rb(acc[m][n][j]);
          qt[(m * 16 + fq * 4 + j) * 40 + n * 16 + fr] = f2bf(phi_f(t));
        }
    f32x4 kv[2][2];
    float zs[2] = {0.f, 0.f};
#pragma unroll
    for (int a = 0; a < 2; ++a)
#pragma unroll
      for (int b = 0; b < 2; ++b) kv[a][b] = f32x4{0.f, 0.f, 0.f, 0.f};
#pragma unroll
    for (int mp = 0; mp < 2; ++mp) {
      bf16x8 pk[2], vv[2];
#pragma unroll
      for (int t = 0; t < 2; ++t)
#pragma unroll
        for (int hh = 0; hh < 2; ++hh)
#pragma unroll
          for (int j = 0; j < 4; ++j) {
            float kk_ = phi_f(rb(acc[2 * mp + hh][2 + t][j]));
            zs[t] += kk_;
            pk[t][hh * 4 + j] = (short)f2bf(kk_);
            vv[t][hh * 4 + j] = (short)f2bf(acc[2 * mp + hh][4 + t][j]);
          }
#pragma unroll
      for (int a = 0; a < 2; ++a)
#pragma unroll
        for (int b = 0; b < 2; ++b) kv[a][b] = __builtin_amdgcn_mfma_f32_16x16x32_bf16(pk[a], vv[b], kv[a][b], 0, 0, 0);
    }
#pragma unroll
    for (int t = 0; t < 2; ++t) { zs[t] += __shfl_xor(zs[t], 16); zs[t] += __shfl_xor(zs[t], 32); }
    asm volatile("s_waitcnt lgkmcnt(0)" ::: "memory");
#pragma unroll
    for (int i = 0; i < 4; ++i) {
      int c = i * 64 + lane, r = c >> 2, ch = c & 3;
      bf16x8 v = *(const bf16x8*)(qt + r * 40 + ch * 8);
      *(bf16x8*)(out + (size_t)(brow + wm * 64 + r) * 1024 + h * 32 + ch * 8) = v;
    }
    float* red = (float*)smem;
    if (wm == 1) {
#pragma unroll
      for (int a = 0; a < 2; ++a)
#pragma unroll
        for (int b = 0; b < 2; ++b)
#pragma unroll
          for (int j = 0; j < 4; ++j) red[wn * 1056 + (a * 16 + fq * 4 + j) * 32 + b * 16 + fr] = kv[a][b][j];
      if (fq == 0) { red[wn * 1056 + 1024 + fr] = zs[0]; red[wn * 1056 + 1024 + 16 + fr] = zs[1]; }
    }
    __syncthreads();
    if (wm == 0) {
      float* dst = part + ((size_t)panel * 32 + h) * 1056;
#pragma unroll
      for (int a = 0; a < 2; ++a)
#pragma unroll
        for (int b = 0; b < 2; ++b)
#pragma unroll
          for (int j = 0; j < 4; ++j) {
            int idx = (a * 16 + fq * 4 + j) * 32 + b * 16 + fr;
            dst[idx] = kv[a][b][j] + red[wn * 1056 + idx];
          }
      if (fq == 0) {
        dst[1024 + fr] = zs[0] + red[wn * 1056 + 1024 + fr];
        dst[1024 + 16 + fr] = zs[1] + red[wn * 1056 + 1024 + 16 + fr];
      }
    }
  } else {
    u16* qt = (u16*)smem + wid * 64 * 40;
    bf16x8 rs[4];
#pragma unroll
    for (int i = 0; i < 4; ++i) {
      int c = i * 64 + lane, r = c >> 2, ch = c & 3;
      rs[i] = *(const bf16x8*)(resid + (size_t)(brow + wm * 64 + r) * 1024 + bcol + wn * 32 + ch * 8);
    }
#pragma unroll
    for (int m = 0; m < 4; ++m)
#pragma unroll
      for (int n = 0; n < 2; ++n)
#pragma unroll
        for (int j = 0; j < 4; ++j) qt[(m * 16 + fq * 4 + j) * 40 + n * 16 + fr] = f2bf(acc[m][n][j]);
    asm volatile("s_waitcnt lgkmcnt(0)" ::: "memory");
#pragma unroll
    for (int i = 0; i < 4; ++i) {
      int c = i * 64 + lane, r = c >> 2, ch = c & 3;
      bf16x8 v = *(const bf16x8*)(qt + r * 40 + ch * 8), o8;
#pragma unroll
      for (int e = 0; e < 8; ++e) o8[e] = (short)f2bf(bf2f((u16)rs[i][e]) + bf2f((u16)v[e]));
      *(bf16x8*)(out + (size_t)(brow + wm * 64 + r) * 1024 + bcol + wn * 32 + ch * 8) = o8;
    }
  }
}

__global__ __launch_bounds__(256) void k_attn_apply(const float* __restrict__ part, const u16* __restrict__ pq,
                                                     u16* __restrict__ o) {
  __shared__ __attribute__((aligned(16))) u16 kvs[1056 + 8];
  __shared__ __attribute__((aligned(16))) u16 ot[4][64 * 40];
  int h = blockIdx.x & 31, chunk = blockIdx.x >> 5;
  int tid = threadIdx.x, wid = tid >> 6, lane = tid & 63, fr = lane & 15, fq = lane >> 4;
  bf16x8 a[4];
#pragma unroll
  for (int m = 0; m < 4; ++m)
    a[m] = *(const bf16x8*)(pq + (size_t)(chunk * 256 + wid * 64 + m * 16 + fr) * 1024 + h * 32 + fq * 8);
  {
    const float4* p4 = (const float4*)(part + (size_t)h * 1056);
    float4 v[16];
#pragma unroll
    for (int p = 0; p < 16; ++p) v[p] = p4[(size_t)p * 32 * 264 + tid];
    float4 w[16];
    if (tid < 8) {
#pragma unroll
      for (int p = 0; p < 16; ++p) w[p] = p4[(size_t)p * 32 * 264 + 256 + tid];
    }
    float4 s = v[0];
#pragma unroll
    for (int p = 1; p < 16; ++p) { s.x += v[p].x; s.y += v[p].y; s.z += v[p].z; s.w += v[p].w; }
    kvs[tid * 4 + 0] = f2bf(s.x); kvs[tid * 4 + 1] = f2bf(s.y); kvs[tid * 4 + 2] = f2bf(s.z); kvs[tid * 4 + 3] = f2bf(s.w);
    if (tid < 8) {
      s = w[0];
#pragma unroll
      for (int p = 1; p < 16; ++p) { s.x += w[p].x; s.y += w[p].y; s.z += w[p].z; s.w += w[p].w; }
      int i = 1024 + tid * 4;
      kvs[i + 0] = f2bf(s.x); kvs[i + 1] = f2bf(s.y); kvs[i + 2] = f2bf(s.z); kvs[i + 3] = f2bf(s.w);
    }
  }
  __syncthreads();
  bf16x8 b0, b1, bz;
#pragma unroll
  for (int j = 0; j < 8; ++j) {
    b0[j] = (short)kvs[(fq * 8 + j) * 32 + fr];
    b1[j] = (short)kvs[(fq * 8 + j) * 32 + 16 + fr];
    bz[j] = (short)kvs[1024 + fq * 8 + j];
  }
  u16* my = ot[wid];
#pragma unroll
  for (int m = 0; m < 4; ++m) {
    f32x4 z4 = {0.f, 0.f, 0.f, 0.f};
    f32x4 n0 = __builtin_amdgcn_mfma_f32_16x16x32_bf16(a[m], b0, z4, 0, 0, 0);
    f32x4 n1 = __builtin_amdgcn_mfma_f32_16x16x32_bf16(a[m], b1, z4, 0, 0, 0);
    f32x4 dn = __builtin_amdgcn_mfma_f32_16x16x32_bf16(a[m], bz, z4, 0, 0, 0);
#pragma unroll
    for (int j = 0; j < 4; ++j) {
      float den = rb(rb(dn[j]) + 1e-6f);
      int r = m * 16 + fq * 4 + j;
      my[r * 40 + fr] = f2bf(rb(n0[j]) / den);
      my[r * 40 + 16 + fr] = f2bf(rb(n1[j]) / den);
    }
  }
  __syncthreads();
#pragma unroll
  for (int i = 0; i < 4; ++i) {
    int c = i * 64 + lane, r = c >> 2, ch = c & 3;
    bf16x8 v = *(const bf16x8*)(my + r * 40 + ch * 8);
    *(bf16x8*)(o + (size_t)(chunk * 256 + wid * 64 + r) * 1024 + h * 32 + ch * 8) = v;
  }
}

__global__ __launch_bounds__(256) void k_row(const u16* __restrict__ x1, const u16* __restrict__ w_r,
                                             u16* __restrict__ xn, u16* __restrict__ x2, u16* __restrict__ xn2,
                                             float* __restrict__ r) {
  int row = blockIdx.x * 4 + (threadIdx.x >> 6), lane = threadIdx.x & 63;
  float v[16], o[16], w[16];
  load_row(x1 + (size_t)row * D_, lane, v);
  load_row(w_r, lane, w);
  ln_row(v, o);
  float s = 0.f;
#pragma unroll
  for (int i = 0; i < 16; ++i) s += o[i] * w[i];
  s = wave_sum(s);
  if (lane == 0) r[row] = rb(s);
  store_row(xn + (size_t)row * D_, lane, o);
#pragma unroll
  for (int i = 0; i < 16; ++i) v[i] = rb(v[i] + o[i]);
  store_row(x2 + (size_t)row * D_, lane, v);
  if (xn2) {
    ln_row(v, o);
    store_row(xn2 + (size_t)row * D_, lane, o);
  }
}

__device__ __forceinline__ void wave_top8(const float* __restrict__ r, int lane, int* out) {
  unsigned key[32];
#pragma unroll
  for (int i = 0; i < 32; ++i) {
    unsigned u = __float_as_uint(r[i * 64 + lane]) >> 16;
    u = (u & 0x8000u) ? (~u & 0xffffu) : (u | 0x8000u);
    key[i] = (u << 11) | (unsigned)(2047 - (i * 64 + lane));
  }
  unsigned best = key[0];
#pragma unroll
  for (int i = 1; i < 32; ++i) best = best > key[i] ? best : key[i];
#pragma unroll
  for (int t = 0; t < 8; ++t) {
    unsigned m = best;
#pragma unroll
    for (int o = 32; o >= 1; o >>= 1) { unsigned om = __shfl_xor(m, o); m = m > om ? m : om; }
    out[t] = 2047 - (int)(m & 2047u);
    if (best == m && t < 7) {
      best = 0;
#pragma unroll
      for (int i = 0; i < 32; ++i) { unsigned k = key[i] == m ? 0u : key[i]; key[i] = k; best = best > k ? best : k; }
    }
  }
}

__global__ __launch_bounds__(256) void k_gateup(const float* __restrict__ r, const u16* __restrict__ xn,
                                                const u16* __restrict__ wg, const u16* __restrict__ wu,
                                                float* __restrict__ gpart, float* __restrict__ upart,
                                                int* __restrict__ idx_out) {
  __shared__ __attribute__((aligned(16))) char smem[2 * 256 * 128];
  int b = blockIdx.x;
  int cg = b % 56, ks = b / 56;
  int tid = threadIdx.x, wid = tid >> 6, lane = tid & 63, fr = lane & 15, fq = lane >> 4;
#pragma unroll
  for (int i = 0; i < 8; ++i) {
    int bb = (i * 256 + tid) * 16, k = bb >> 7, cp = (bb >> 5) & 3, half = (bb >> 4) & 1;
    int sw = ((k >> 1) & 1) | (((k >> 3) & 1) << 1);
    int c = cp ^ sw;
    size_t src = (size_t)(ks * 256 + k) * F_ + cg * 64 + c * 16 + half * 8;
    __builtin_amdgcn_global_load_lds((const unsigned*)(wg + src), (unsigned*)(smem + bb), 16, 0, 0);
    __builtin_amdgcn_global_load_lds((const unsigned*)(wu + src), (unsigned*)(smem + 32768 + bb), 16, 0, 0);
  }
  int idx[8];
  wave_top8(r, lane, idx);
  if (b == 0 && tid < 8) {
    int v = idx[0];
#pragma unroll
    for (int i = 1; i < 8; ++i) if (tid == i) v = idx[i];
    idx_out[tid] = v;
  }
  int myrow = idx[0];
#pragma unroll
  for (int i = 1; i < 8; ++i) if ((fr & 7) == i) myrow = idx[i];
  f32x4 acc[2][4];
#pragma unroll
  for (int a = 0; a < 2; ++a)
#pragma unroll
    for (int n = 0; n < 4; ++n) acc[a][n] = f32x4{0.f, 0.f, 0.f, 0.f};
  bf16x8 af[2];
#pragma unroll
  for (int kk = 0; kk < 2; ++kk) af[kk] = *(const bf16x8*)(xn + (size_t)myrow * D_ + ks * 256 + wid * 64 + kk * 32 + fq * 8);
  asm volatile("s_waitcnt vmcnt(0)" ::: "memory");
  __syncthreads();
  const int g = lane >> 4, q = (lane & 15) >> 2, p = lane & 3;
#pragma unroll
  for (int kk = 0; kk < 2; ++kk)
#pragma unroll
    for (int a = 0; a < 2; ++a)
#pragma unroll
      for (int n = 0; n < 4; ++n) {
        int k = wid * 64 + kk * 32 + 8 * g + q;
        int sw = ((k >> 1) & 1) | (((k >> 3) & 1) << 1);
        s16x4 lo = vtr((lds_cptr)(smem + a * 32768 + k * 128 + ((n ^ sw) << 5) + p * 8));
        k += 4;
        sw = ((k >> 1) & 1) | (((k >> 3) & 1) << 1);
        s16x4 hi = vtr((lds_cptr)(smem + a * 32768 + k * 128 + ((n ^ sw) << 5) + p * 8));
        bf16x8 bf = bf16x8{lo[0], lo[1], lo[2], lo[3], hi[0], hi[1], hi[2], hi[3]};
        acc[a][n] = __builtin_amdgcn_mfma_f32_16x16x32_bf16(af[kk], bf, acc[a][n], 0, 0, 0);
      }
  __syncthreads();
  float* red = (float*)smem;
  if (fq < 2) {
#pragma unroll
    for (int a = 0; a < 2; ++a)
#pragma unroll
      for (int n = 0; n < 4; ++n)
#pragma unroll
        for (int j = 0; j < 4; ++j) red[((wid * 2 + a) * 8 + fq * 4 + j) * 64 + n * 16 + fr] = acc[a][n][j];
  }
  __syncthreads();
  for (int i = tid; i < 1024; i += 256) {
    int a = i >> 9, row = (i >> 6) & 7, c = i & 63;
    float s = 0.f;
#pragma unroll
    for (int w = 0; w < 4; ++w) s += red[((w * 2 + a) * 8 + row) * 64 + c];
    float* dst = a ? upart : gpart;
    dst[((size_t)ks * 8 + row) * F_ + cg * 64 + c] = s;
  }
}

__global__ __launch_bounds__(256) void k_down(const float* __restrict__ gpart, const float* __restrict__ upart,
                                              const u16* __restrict__ wd, float* __restrict__ ypart) {
  __shared__ __attribute__((aligned(16))) char smem[256 * 128 + 8 * 256 * 2];
  u16* hs = (u16*)(smem + 32768);
  int b = blockIdx.x;
  int cg = b & 15, kc = b >> 4;
  int tid = threadIdx.x, wid = tid >> 6, lane = tid & 63, fr = lane & 15, fq = lane >> 4;
#pragma unroll
  for (int i = 0; i < 8; ++i) {
    int bb = (i * 256 + tid) * 16, k = bb >> 7, cp = (bb >> 5) & 3, half = (bb >> 4) & 1;
    int sw = ((k >> 1) & 1) | (((k >> 3) & 1) << 1);
    int c = cp ^ sw;
    size_t src = (size_t)(kc * 256 + k) * D_ + cg * 64 + c * 16 + half * 8;
    __builtin_amdgcn_global_load_lds((const unsigned*)(wd + src), (unsigned*)(smem + bb), 16, 0, 0);
  }
  {
    float4 gv[2][4], uv[2][4];
#pragma unroll
    for (int i = 0; i < 2; ++i) {
      int e = (i * 256 + tid) * 4, row = e >> 8, c = e & 255;
#pragma unroll
      for (int sidx = 0; sidx < 4; ++sidx) {
        gv[i][sidx] = *(const float4*)(gpart + ((size_t)sidx * 8 + row) * F_ + kc * 256 + c);
        uv[i][sidx] = *(const float4*)(upart + ((size_t)sidx * 8 + row) * F_ + kc * 256 + c);
      }
    }
#pragma unroll
    for (int i = 0; i < 2; ++i) {
      float g4[4] = {gv[i][0].x + gv[i][1].x + gv[i][2].x + gv[i][3].x, gv[i][0].y + gv[i][1].y + gv[i][2].y + gv[i][3].y,
                     gv[i][0].z + gv[i][1].z + gv[i][2].z + gv[i][3].z, gv[i][0].w + gv[i][1].w + gv[i][2].w + gv[i][3].w};
      float u4[4] = {uv[i][0].x + uv[i][1].x + uv[i][2].x + uv[i][3].x, uv[i][0].y + uv[i][1].y + uv[i][2].y + uv[i][3].y,
                     uv[i][0].z + uv[i][1].z + uv[i][2].z + uv[i][3].z, uv[i][0].w + uv[i][1].w + uv[i][2].w + uv[i][3].w};
      int e = (i * 256 + tid) * 4;
#pragma unroll
      for (int j = 0; j < 4; ++j) {
        float gs = rb(g4[j]), us = rb(u4[j]);
        float sl = rb(gs / (1.f + __expf(-gs)));
        hs[e + j] = f2bf(sl * us);
      }
    }
  }
  asm volatile("s_waitcnt vmcnt(0)" ::: "memory");
  __syncthreads();
  f32x4 acc[4];
#pragma unroll
  for (int n = 0; n < 4; ++n) acc[n] = f32x4{0.f, 0.f, 0.f, 0.f};
  const int g = lane >> 4, q = (lane & 15) >> 2, p = lane & 3;
#pragma unroll
  for (int kk = 0; kk < 2; ++kk) {
    bf16x8 af = *(const bf16x8*)(hs + (fr & 7) * 256 + wid * 64 + kk * 32 + fq * 8);
#pragma unroll
    for (int n = 0; n < 4; ++n) {
      int k = wid * 64 + kk * 32 + 8 * g + q;
      int sw = ((k >> 1) & 1) | (((k >> 3) & 1) << 1);
      s16x4 lo = vtr((lds_cptr)(smem + k * 128 + ((n ^ sw) << 5) + p * 8));
      k += 4;
      sw = ((k >> 1) & 1) | (((k >> 3) & 1) << 1);
      s16x4 hi = vtr((lds_cptr)(smem + k * 128 + ((n ^ sw) << 5) + p * 8));
      bf16x8 bf = bf16x8{lo[0], lo[1], lo[2], lo[3], hi[0], hi[1], hi[2], hi[3]};
      acc[n] = __builtin_amdgcn_mfma_f32_16x16x32_bf16(af, bf, acc[n], 0, 0, 0);
    }
  }
  __syncthreads();
  float* red = (float*)smem;
  if (fq < 2) {
#pragma unroll
    for (int n = 0; n < 4; ++n)
#pragma unroll
      for (int j = 0; j < 4; ++j) red[(wid * 8 + fq * 4 + j) * 64 + n * 16 + fr] = acc[n][j];
  }
  __syncthreads();
  for (int i = tid; i < 512; i += 256) {
    int row = i >> 6, c = i & 63;
    float s = red[row * 64 + c] + red[(8 + row) * 64 + c] + red[(16 + row) * 64 + c] + red[(24 + row) * 64 + c];
    ypart[((size_t)kc * 8 + row) * D_ + cg * 64 + c] = s;
  }
}

__global__ __launch_bounds__(64) void k_fix(const float* __restrict__ ypart, const int* __restrict__ idx,
                                            const float* __restrict__ r, const u16* __restrict__ x1,
                                            const u16* __restrict__ xn, u16* __restrict__ x2, u16* __restrict__ xn2) {
  int c = blockIdx.x, lane = threadIdx.x;
  int row = idx[c];
  float rs = rb(1.f / (1.f + __expf(-r[row])));
  float v[16], o[16], y[16];
  load_row(x1 + (size_t)row * D_, lane, v);
  load_row(xn + (size_t)row * D_, lane, o);
#pragma unroll
  for (int i = 0; i < 16; ++i) y[i] = 0.f;
  {
    float4 t[14][4];
#pragma unroll
    for (int kc = 0; kc < 14; ++kc) {
      const float* src = ypart + ((size_t)kc * 8 + c) * D_;
      t[kc][0] = *(const float4*)(src + lane * 8); t[kc][1] = *(const float4*)(src + lane * 8 + 4);
      t[kc][2] = *(const float4*)(src + 512 + lane * 8); t[kc][3] = *(const float4*)(src + 512 + lane * 8 + 4);
    }
#pragma unroll
    for (int kc = 0; kc < 14; ++kc) {
#pragma unroll
      for (int q = 0; q < 4; ++q) { y[q * 4 + 0] += t[kc][q].x; y[q * 4 + 1] += t[kc][q].y; y[q * 4 + 2] += t[kc][q].z; y[q * 4 + 3] += t[kc][q].w; }
    }
  }
#pragma unroll
  for (int i = 0; i < 16; ++i) {
    float t = rb(rb(y[i]) * rs);
    float bo = rb(o[i] + t);
    v[i] = rb(v[i] + bo);
  }
  store_row(x2 + (size_t)row * D_, lane, v);
  if (xn2) {
    ln_row(v, o);
    store_row(xn2 + (size_t)row * D_, lane, o);
  }
}

extern "C" void kernel_launch(void* const* d_in, const int* in_sizes, int n_in,
                              void* d_out, int out_size, void* d_ws, size_t ws_size,
                              hipStream_t stream) {
  const u16* A_x = (const u16*)d_in[0];
  const u16* wqkv = (const u16*)d_in[1];
  const u16* wo = (const u16*)d_in[2];
  const u16* w_r = (const u16*)d_in[4];
  const u16* wg = (const u16*)d_in[5];
  const u16* wu = (const u16*)d_in[6];
  const u16* wd = (const u16*)d_in[7];
  u16* outp = (u16*)d_out;

  char* ws = (char*)d_ws;
  const size_t MB4 = (size_t)S_ * D_ * 2;
  u16* xn = (u16*)(ws);
  u16* pq = (u16*)(ws + MB4);
  u16* o = (u16*)(ws + 2 * MB4);
  u16* x1 = (u16*)(ws + 3 * MB4);
  u16* x2 = (u16*)(ws + 4 * MB4);
  u16* xnb = (u16*)(ws + 5 * MB4);
  char* p = ws + 6 * MB4;
  float* part = (float*)p; p += (size_t)16 * 32 * 1056 * 4;
  float* r = (float*)p; p += 2048 * 4;
  int* idx = (int*)p; p += 256;
  unsigned* cnt = (unsigned*)p; p += 17 * 256;
  float* gpart = (float*)p; p += (size_t)4 * 8 * F_ * 4;
  float* upart = (float*)p; p += (size_t)4 * 8 * F_ * 4;
  float* ypart = (float*)p; p += (size_t)14 * 8 * D_ * 4;

  constexpr int LDS1 = 3 * (16384 + 64 * 384);
  constexpr int LDS3 = 4 * (16384 + 64 * 128);
  hipFuncSetAttribute((const void*)k_gemm<3, 0, 3>, hipFuncAttributeMaxDynamicSharedMemorySize, LDS1);

  hipFuncSetAttribute((const void*)k_gemm<1, 1, 4>, hipFuncAttributeMaxDynamicSharedMemorySize, LDS3);
  k_ln<<<512, 256, 0, stream>>>(A_x, xn);
  const u16* xcur = A_x;
  for (int layer = 0; layer < 2; ++layer) {
    k_gemm<3, 0, 3><<<256, 512, LDS1, stream>>>(xn, wqkv, 3072, pq, nullptr, part);
    k_attn_apply<<<256, 256, 0, stream>>>(part, pq, o);
    k_gemm<1, 1, 4><<<256, 512, LDS3, stream>>>(o, wo, 1024, x1, xcur, nullptr);
    u16* xo = (layer == 1) ? outp : x2;
    u16* xn2 = (layer == 1) ? nullptr : xn;
    k_row<<<512, 256, 0, stream>>>(x1, w_r, xnb, xo, xn2, r);
    k_gateup<<<224, 256, 0, stream>>>(r, xnb, wg, wu, gpart, upart, idx);
    k_down<<<224, 256, 0, stream>>>(gpart, upart, wd, ypart);
    k_fix<<<8, 64, 0, stream>>>(ypart, idx, r, x1, xnb, xo, xn2);
    xcur = x2;
  }
}
```
